```python
import torch

def _softmax(x, axis=-1):
    return torch.softmax(x, dim=axis)
def _gelu(x):
    return torch.nn.functional.gelu(x, approximate="tanh")
def _silu(x):
    return torch.nn.functional.silu(x)
def _sigmoid(x):
    return torch.sigmoid(x)
def _layernorm(x, axis=-1, eps=1e-5):
    m = x.mean(dim=axis, keepdim=True)
    v = x.var(dim=axis, keepdim=True, unbiased=False)
    return (x - m) / torch.sqrt(v + eps)
def _scatter_add(tgt, idx, src, axis=0):
    return tgt.clone().index_add_(axis, idx, src)
def _segment_sum(src, seg, n):
    out = torch.zeros((n,) + src.shape[1:], dtype=src.dtype)
    return out.index_add_(0, seg, src)
def _pad(x, pw):
    flat = tuple(v for pair in reversed(pw) for v in pair)
    return torch.nn.functional.pad(x, flat)

kernel_name = 'standalone__sliding_local_strided__prompt'

def setup_inputs():

    _w = (lambda a: (lambda t: t.to(torch.bfloat16) if t.is_floating_point() else t)(torch.as_tensor(a)))
    B, H, S, D = 1, 32, 2048, 128
    return {
        "q": _w((lambda *s: torch.randn(*s, dtype=torch.bfloat16) * 0.8)(B, H, S, D)),
        "k": _w((lambda *s: torch.randn(*s, dtype=torch.bfloat16) * 0.8)(B, H, S, D)),
        "v": _w((lambda *s: torch.randn(*s, dtype=torch.bfloat16) * 0.05)(B, H, S, D)),
    }

def reference(q, k, v):
    B, H, S, D = q.shape
    W, dil = 8, 4
    scale = D ** -0.5
    pos = torch.arange(S)
    kidx = pos[:, None] - torch.arange(W)[None, :] * dil
    valid = kidx >= 0
    kidx = torch.where(valid, kidx, torch.zeros_like(kidx))
    idx = torch.reshape(kidx, (1, 1, S * W, 1))
    idx = (lambda x, n, axis: torch.repeat_interleave(x, n, dim=axis))(idx, B, 0)
    idx = (lambda x, n, axis: torch.repeat_interleave(x, n, dim=axis))(idx, H, 1)
    idx = (lambda x, n, axis: torch.repeat_interleave(x, n, dim=axis))(idx, D, 3)
    kg = torch.reshape((lambda x, i, axis: torch.take_along_dim(x, i, dim=axis))(k, idx, 2), (B, H, S, W, D))
    vg = torch.reshape((lambda x, i, axis: torch.take_along_dim(x, i, dim=axis))(v, idx, 2), (B, H, S, W, D))
    scores = torch.einsum("bhsd,bhswd->bhsw", q, kg) * scale
    mask_f = (lambda x, dt: x.to(dt))(valid, q.dtype)[None, None, :, :]
    scores = scores + (mask_f - 1.0) * 1e9
    p = _softmax(scores, axis=-1)
    return torch.einsum("bhsw,bhswd->bhsd", p, vg)
```

```cpp
#include <hip/hip_runtime.h>
#include <hip/hip_bf16.h>
#include <stdint.h>

#define T_STEPS 8
#define WPB 4
#define AUX_Q 0
#define AUX_KV 0
#define AUX_O 0
#define SWZ 1
#define MINW 2

typedef unsigned u32;
typedef __attribute__((ext_vector_type(4))) unsigned u32x4;
typedef __attribute__((ext_vector_type(2))) __bf16 bf16x2;

constexpr int HEADS = 32, SEQ = 2048, DIM = 128, WIN = 8;
constexpr int STEPS = SEQ / 4;
constexpr int T = T_STEPS;
constexpr int CHUNKS_PER_HEAD = STEPS / T;
constexpr int NCHUNK = HEADS * CHUNKS_PER_HEAD;
constexpr int NBLK = NCHUNK / WPB;
constexpr u32 TENSOR_BYTES = (u32)HEADS * SEQ * DIM * 2;
constexpr float SCALE_LOG2E = 0.08838834764831845f * 1.4426950408889634f;
constexpr float MASK_LOG2E = -1.0e9f * 1.4426950408889634f;

__device__ __forceinline__ float dot2(u32 a, u32 b, float c) {
  return __builtin_amdgcn_fdot2_f32_bf16(__builtin_bit_cast(bf16x2, a), __builtin_bit_cast(bf16x2, b), c, false);
}
template <int CTRL>
__device__ __forceinline__ float dpp(float x) {
  return __builtin_bit_cast(float, __builtin_amdgcn_mov_dpp(__builtin_bit_cast(int, x), CTRL, 0xf, 0xf, true));
}
template <int CTRL>
__device__ __forceinline__ u32 dppu(u32 x) {
  return (u32)__builtin_amdgcn_mov_dpp((int)x, CTRL, 0xf, 0xf, true);
}
__device__ __forceinline__ u32 pack_bf16(float lo, float hi) {
  typedef __attribute__((ext_vector_type(2))) float f32x2;
  f32x2 f = {lo, hi};
  return __builtin_bit_cast(u32, __builtin_convertvector(f, bf16x2));
}
constexpr int XOR1 = 0xB1, XOR2 = 0x4E, HMIR = 0x141, ROR8 = 0x128, BCAST = 0x150;

__global__ __launch_bounds__(WPB * 64, MINW) void sliding_kernel(const void* __restrict__ q, const void* __restrict__ k,
                                                                 const void* __restrict__ v, void* __restrict__ o) {
  const int lane = threadIdx.x & 63;
  const int c = lane & 15;
  const int wave = __builtin_amdgcn_readfirstlane(threadIdx.x >> 6);
  int b = blockIdx.x;
  b = (b & 7) * (NBLK / 8) + (b >> 3);
  const int chunk = b * WPB + wave;
  const int j0 = (chunk % CHUNKS_PER_HEAD) * T;
  const u32 sbase = (u32)chunk * (T * 1024u);

  const auto rq = __builtin_amdgcn_make_buffer_rsrc((void*)q, 0, TENSOR_BYTES, 0x00020000);
  const auto rk = __builtin_amdgcn_make_buffer_rsrc((void*)k, 0, TENSOR_BYTES, 0x00020000);
  const auto rv = __builtin_amdgcn_make_buffer_rsrc((void*)v, 0, TENSOR_BYTES, 0x00020000);
  const auto ro = __builtin_amdgcn_make_buffer_rsrc(o, 0, TENSOR_BYTES, 0x00020000);

  const bool first = (j0 == 0);
  const u32 voff = lane * 16;
  const u32 voffh = first ? (u32)(c * 16) : voff;

  u32x4 K[T + 7], V[T + 7], Q[T];
#pragma unroll
  for (int i = 0; i < 7; ++i) {
    const u32 so = first ? sbase : sbase - (7 - i) * 1024u;
    K[i] = __builtin_amdgcn_raw_buffer_load_b128(rk, voffh, so, AUX_KV);
  }
  K[7] = __builtin_amdgcn_raw_buffer_load_b128(rk, voff, sbase, AUX_KV);
  Q[0] = __builtin_amdgcn_raw_buffer_load_b128(rq, voff, sbase, AUX_Q);
#pragma unroll
  for (int i = 0; i < 7; ++i) {
    const u32 so = first ? sbase : sbase - (7 - i) * 1024u;
    V[i] = __builtin_amdgcn_raw_buffer_load_b128(rv, voffh, so, AUX_KV);
  }
  V[7] = __builtin_amdgcn_raw_buffer_load_b128(rv, voff, sbase, AUX_KV);
#pragma unroll
  for (int t = 1; t < T; ++t) {
    K[7 + t] = __builtin_amdgcn_raw_buffer_load_b128(rk, voff, sbase + t * 1024u, AUX_KV);
    Q[t] = __builtin_amdgcn_raw_buffer_load_b128(rq, voff, sbase + t * 1024u, AUX_Q);
    V[7 + t] = __builtin_amdgcn_raw_buffer_load_b128(rv, voff, sbase + t * 1024u, AUX_KV);
  }
  __builtin_amdgcn_sched_barrier(0);

  const int wl = c >> 1;
  u32 P[T + 7][8];

#pragma unroll
  for (int t = 0; t < T; ++t) {
    float x[8];
#pragma unroll
    for (int w = 0; w < 8; ++w) {
      const u32x4 kk = K[7 + t - w];
      float d = dot2(kk[0], Q[t][0], 0.f);
      d = dot2(kk[1], Q[t][1], d);
      d = dot2(kk[2], Q[t][2], d);
      d = dot2(kk[3], Q[t][3], d);
      x[w] = d;
    }
    float y[4], z[2];
#pragma unroll
    for (int i = 0; i < 4; ++i) {
      const float a = x[i] + dpp<ROR8>(x[i]), bb = x[i + 4] + dpp<ROR8>(x[i + 4]);
      y[i] = (c & 8) ? bb : a;
    }
#pragma unroll
    for (int i = 0; i < 2; ++i) {
      const float a = y[i] + dpp<HMIR>(y[i]), bb = y[i + 2] + dpp<HMIR>(y[i + 2]);
      z[i] = (c & 4) ? bb : a;
    }
    const float a = z[0] + dpp<XOR2>(z[0]), bb = z[1] + dpp<XOR2>(z[1]);
    float s = (c & 2) ? bb : a;
    s += dpp<XOR1>(s);
    s *= SCALE_LOG2E;
    if (t < 7) s += (wl > j0 + t) ? MASK_LOG2E : 0.f;
    float m = fmaxf(s, dpp<XOR2>(s));
    m = fmaxf(m, dpp<HMIR>(m));
    m = fmaxf(m, dpp<ROR8>(m));
    const float e = __builtin_amdgcn_exp2f(s - m);
    float l = e + dpp<XOR2>(e);
    l += dpp<HMIR>(l);
    l += dpp<ROR8>(l);
    const float p = e * __builtin_amdgcn_rcpf(l);
    const u32 pk = pack_bf16(p, dpp<XOR2>(p));

    const int ms = (t == 0) ? 1 : (t == 1 ? 1 : 7 + t);
#pragma unroll
    for (int m2 = 1; m2 <= 7 + t; ++m2) {
      const bool need_now = (t == 0) ? (m2 == 7 || m2 == 5 || m2 == 3 || m2 == 1)
                                     : (t == 1 ? (m2 == 8 || m2 == 6 || m2 == 4 || m2 == 2) : (m2 == 7 + t));
      if (need_now) {
#pragma unroll
        for (int i = 0; i < 4; ++i) {
          P[m2][2 * i] = __builtin_amdgcn_perm(V[m2 - 1][i], V[m2][i], 0x05040100u);
          P[m2][2 * i + 1] = __builtin_amdgcn_perm(V[m2 - 1][i], V[m2][i], 0x07060302u);
        }
      }
    }
    (void)ms;
    const u32 p01 = dppu<BCAST + 0>(pk), p23 = dppu<BCAST + 4>(pk), p45 = dppu<BCAST + 8>(pk),
              p67 = dppu<BCAST + 12>(pk);
    float acc[8];
#pragma unroll
    for (int e2 = 0; e2 < 8; ++e2) {
      float a2 = dot2(P[7 + t][e2], p01, 0.f);
      a2 = dot2(P[5 + t][e2], p23, a2);
      a2 = dot2(P[3 + t][e2], p45, a2);
      a2 = dot2(P[1 + t][e2], p67, a2);
      acc[e2] = a2;
    }
    u32x4 out;
#pragma unroll
    for (int i = 0; i < 4; ++i) out[i] = pack_bf16(acc[2 * i], acc[2 * i + 1]);
    __builtin_amdgcn_raw_buffer_store_b128(out, ro, voff, sbase + t * 1024u, AUX_O);
  }
}

extern "C" void kernel_launch(void* const* d_in, const int* in_sizes, int n_in, void* d_out, int out_size, void* d_ws,
                              size_t ws_size, hipStream_t stream) {
  sliding_kernel<<<NBLK, WPB * 64, 0, stream>>>(d_in[0], d_in[1], d_in[2], d_out);
}
```

```cpp
#include <hip/hip_runtime.h>
#include <hip/hip_bf16.h>
#include <stdint.h>


typedef unsigned u32;
typedef __attribute__((ext_vector_type(4))) unsigned u32x4;
typedef __attribute__((ext_vector_type(2))) unsigned u32x2;
typedef __attribute__((ext_vector_type(4))) float f32x4;
typedef __attribute__((ext_vector_type(2))) float f32x2;
typedef __attribute__((ext_vector_type(2))) __bf16 bf16x2;
typedef __attribute__((ext_vector_type(8))) __bf16 bf16x8;
typedef __attribute__((ext_vector_type(4))) short s16x4;
#define LDS_AS __attribute__((address_space(3)))
#define DI __device__ __forceinline__

constexpr int HEADS = 32, SEQ = 2048, DIM = 128;
constexpr u32 TENSOR_BYTES = (u32)HEADS * SEQ * DIM * 2;
constexpr int NBLK = 256;
constexpr int TILE_B = 4096;
constexpr int OFF_K = 0, OFF_V = 3 * TILE_B, OFF_Q = 6 * TILE_B, OFF_O = 8 * TILE_B, WAVE_LDS = 9 * TILE_B;
constexpr u32 TILE_G = 16384;
constexpr u32 OOB = 0x40000000u;
constexpr float SCALE_LOG2E = 0.08838834764831845f * 1.4426950408889634f;
constexpr float MASK_LOG2E = -1.0e9f * 1.4426950408889634f;
constexpr int AUX_SC1 = 16;

constexpr int PRE = 3;
constexpr int group_size(int g) { return g == 0 ? 12 : (g == 1 ? 8 : ((g & 1) ? 4 : 8)); }
constexpr int younger_ops(int c) {
  int n = 0;
  const int last = (PRE + c - 1 < 7) ? PRE + c - 1 : 7;
  for (int g = c + 1; g <= last; ++g) n += group_size(g);
  for (int i = 0; i < 4; ++i)
    if (2 * i + 1 < c && c - PRE <= 2 * i + 1) n += 4;
  return n;
}

DI u32 pack_bf16(float lo, float hi) {
  f32x2 f = {lo, hi};
  return __builtin_bit_cast(u32, __builtin_convertvector(f, bf16x2));
}
DI f32x4 mfma(u32x4 a, u32x4 b, f32x4 c) {
  return __builtin_amdgcn_mfma_f32_16x16x32_bf16(__builtin_bit_cast(bf16x8, a), __builtin_bit_cast(bf16x8, b), c, 0, 0,
                                                 0);
}
DI u32x4 lds_read128(u32 addr) { return *(const LDS_AS u32x4*)(size_t)addr; }
DI void lds_write128(u32 addr, u32x4 v) { *(LDS_AS u32x4*)(size_t)addr = v; }
DI u32x2 lds_read_tr(u32 addr) {
  return __builtin_bit_cast(u32x2, __builtin_amdgcn_ds_read_tr16_b64_v4i16((LDS_AS s16x4*)(size_t)addr));
}
DI float max_across_groups(float x) {
  auto s = __builtin_amdgcn_permlane16_swap(__float_as_uint(x), __float_as_uint(x), false, false);
  x = fmaxf(__uint_as_float(s[0]), __uint_as_float(s[1]));
  auto t = __builtin_amdgcn_permlane32_swap(__float_as_uint(x), __float_as_uint(x), false, false);
  return fmaxf(__uint_as_float(t[0]), __uint_as_float(t[1]));
}
DI float sum_across_groups(float x) {
  auto s = __builtin_amdgcn_permlane16_swap(__float_as_uint(x), __float_as_uint(x), false, false);
  x = __uint_as_float(s[0]) + __uint_as_float(s[1]);
  auto t = __builtin_amdgcn_permlane32_swap(__float_as_uint(x), __float_as_uint(x), false, false);
  return __uint_as_float(t[0]) + __uint_as_float(t[1]);
}
template <int N>
DI void wait_vm() {
  asm volatile("s_waitcnt vmcnt(%0)" ::"n"(N) : "memory");
}

#define DMA_TILE(ldsbase, v0, v1, v2, v3, rsrc, soff)                                                       \
  asm volatile("s_mov_b32 m0, %0\n\ts_nop 0\n\t"                                                            \
               "buffer_load_dwordx4 %1, %5, %6 offen nt lds\n\t"                                            \
               "buffer_load_dwordx4 %2, %5, %6 offen offset:1024 nt lds\n\t"                                \
               "buffer_load_dwordx4 %3, %5, %6 offen offset:2048 nt lds\n\t"                                \
               "buffer_load_dwordx4 %4, %5, %6 offen offset:3072 nt lds"                                    \
               ::"s"(ldsbase), "v"(v0), "v"(v1), "v"(v2), "v"(v3), "s"(rsrc), "s"(soff)                     \
               : "memory")
#define DMA_ROWS4(ldsbase, vo, rsrc, soff)                                                                  \
  asm volatile("s_mov_b32 m0, %0\n\ts_nop 0\n\t"                                                            \
               "buffer_load_dwordx4 %1, %2, %3 offen nt lds" ::"s"(ldsbase), "v"(vo), "s"(rsrc), "s"(soff)  \
               : "memory")

struct Wave {
  u32 lbase;
  u32 x0base;
  u32 xbase, xstep;
  u32 qbase;
  u32 vo_out;
  u32x4 rq, rk, rv;
  __amdgpu_buffer_rsrc_t ro;
  u32 vo_k[4], vo_v[4];
  u32 x0_k[4], x0_v[4];
  u32 x4_k[2], x4_v[2];
  u32 a_row[4], a_tr[4], a_ow[2], a_or[2];
  float bias_new[4], bias_old[4], bias_old0[4];
  u32x4 kf[2][4];
  u32x4 vf[8];
};

template <int J>
DI void dma_k(Wave& w) {
  const u32 l = w.lbase + OFF_K + (J % 3) * TILE_B, s = w.xbase + J * w.xstep;
  if constexpr (J == 4) DMA_TILE(l, w.x4_k[0], w.x4_k[1], w.vo_k[2], w.vo_k[3], w.rk, s);
  else DMA_TILE(l, w.vo_k[0], w.vo_k[1], w.vo_k[2], w.vo_k[3], w.rk, s);
}
template <int J>
DI void dma_v(Wave& w) {
  const u32 l = w.lbase + OFF_V + (J % 3) * TILE_B, s = w.xbase + J * w.xstep;
  if constexpr (J == 4) DMA_TILE(l, w.x4_v[0], w.x4_v[1], w.vo_v[2], w.vo_v[3], w.rv, s);
  else DMA_TILE(l, w.vo_v[0], w.vo_v[1], w.vo_v[2], w.vo_v[3], w.rv, s);
}
template <int I>
DI void dma_q(Wave& w) {
  DMA_TILE(w.lbase + OFF_Q + (I & 1) * TILE_B, w.vo_k[0], w.vo_k[1], w.vo_k[2], w.vo_k[3], w.rq,
           w.qbase + I * w.xstep);
}

template <int G>
DI void issue_group(Wave& w) {
  if constexpr (G == 0) {
#pragma unroll
    for (int m = 0; m < 4; ++m) DMA_ROWS4(w.lbase + OFF_K + 1024 * m, w.x0_k[m], w.rk, w.x0base);
    dma_k<1>(w);
    dma_q<0>(w);
  } else if constexpr (G == 1) {
#pragma unroll
    for (int m = 0; m < 4; ++m) DMA_ROWS4(w.lbase + OFF_V + 1024 * m, w.x0_v[m], w.rv, w.x0base);
    dma_v<1>(w);
  } else if constexpr (G <= 7) {
    if constexpr (G & 1) {
      dma_v<(G + 1) / 2>(w);
    } else {
      dma_k<G / 2 + 1>(w);
      dma_q<G / 2>(w);
    }
  }
}

template <int T>
DI void tile(Wave& w) {
  constexpr int NEW = (T + 1) & 1, OLD = NEW ^ 1;
  constexpr int LK = OFF_K + ((T + 1) % 3) * TILE_B, LV = ((T + 1) % 3) * TILE_B, LQ = OFF_Q + (T & 1) * TILE_B;

  wait_vm<younger_ops(2 * T)>();
  issue_group<PRE + 2 * T>(w);
  u32x4 qf[4];
#pragma unroll
  for (int x = 0; x < 4; ++x) qf[x] = lds_read128(w.a_row[x] + LQ);
  if constexpr (T == 0) {
#pragma unroll
    for (int x = 0; x < 4; ++x) w.kf[OLD][x] = lds_read128(w.a_row[x] + OFF_K);
  }
#pragma unroll
  for (int x = 0; x < 4; ++x) w.kf[NEW][x] = lds_read128(w.a_row[x] + LK);
  __builtin_amdgcn_sched_barrier(0);
  f32x4 s_old = {0.f, 0.f, 0.f, 0.f}, s_new = {0.f, 0.f, 0.f, 0.f};
#pragma unroll
  for (int x = 0; x < 4; ++x) {
    s_old = mfma(w.kf[OLD][x], qf[x], s_old);
    s_new = mfma(w.kf[NEW][x], qf[x], s_new);
  }

  float tn[4], to[4];
#pragma unroll
  for (int e = 0; e < 4; ++e) {
    to[e] = fmaf(s_old[e], SCALE_LOG2E, T == 0 ? w.bias_old0[e] : w.bias_old[e]);
    tn[e] = fmaf(s_new[e], SCALE_LOG2E, w.bias_new[e]);
  }
  float mx = fmaxf(fmaxf(fmaxf(to[0], to[1]), fmaxf(to[2], to[3])), fmaxf(fmaxf(tn[0], tn[1]), fmaxf(tn[2], tn[3])));
  mx = max_across_groups(mx);
#pragma unroll
  for (int e = 0; e < 4; ++e) {
    to[e] = __builtin_amdgcn_exp2f(to[e] - mx);
    tn[e] = __builtin_amdgcn_exp2f(tn[e] - mx);
  }
  const float inv = __builtin_amdgcn_rcpf(
      sum_across_groups(((to[0] + to[1]) + (to[2] + to[3])) + ((tn[0] + tn[1]) + (tn[2] + tn[3]))));
#pragma unroll
  for (int e = 0; e < 4; ++e) {
    to[e] *= inv;
    tn[e] *= inv;
  }
  u32x4 pf;
  pf[2 * OLD] = pack_bf16(to[0], to[1]);
  pf[2 * OLD + 1] = pack_bf16(to[2], to[3]);
  pf[2 * NEW] = pack_bf16(tn[0], tn[1]);
  pf[2 * NEW + 1] = pack_bf16(tn[2], tn[3]);
  asm volatile("" : "+v"(pf));

  wait_vm<younger_ops(2 * T + 1)>();
  issue_group<PRE + 2 * T + 1>(w);
  if constexpr (T == 0) {
#pragma unroll
    for (int cb = 0; cb < 8; ++cb) {
      const u32x2 x = lds_read_tr(w.a_tr[cb >> 1] + 8 * (cb & 1));
      w.vf[cb][2 * OLD] = x[0];
      w.vf[cb][2 * OLD + 1] = x[1];
    }
  }
#pragma unroll
  for (int cb = 0; cb < 8; ++cb) {
    const u32x2 x = lds_read_tr(w.a_tr[cb >> 1] + LV + 8 * (cb & 1));
    w.vf[cb][2 * NEW] = x[0];
    w.vf[cb][2 * NEW + 1] = x[1];
  }
  __builtin_amdgcn_sched_barrier(0);
  const f32x4 zero = {0.f, 0.f, 0.f, 0.f};
  f32x4 acc[8];
#pragma unroll
  for (int cb = 0; cb < 8; ++cb) acc[cb] = mfma(w.vf[cb], pf, zero);

#pragma unroll
  for (int cc = 0; cc < 4; ++cc) {
    u32x4 x;
    x[0] = pack_bf16(acc[2 * cc][0], acc[2 * cc][1]);
    x[1] = pack_bf16(acc[2 * cc][2], acc[2 * cc][3]);
    x[2] = pack_bf16(acc[2 * cc + 1][0], acc[2 * cc + 1][1]);
    x[3] = pack_bf16(acc[2 * cc + 1][2], acc[2 * cc + 1][3]);
    lds_write128(w.a_ow[cc & 1] + 128 * (cc >> 1), x);
  }
  u32x4 ob[4];
#pragma unroll
  for (int n = 0; n < 4; ++n) ob[n] = lds_read128(w.a_or[n & 1] + 1024 * n);
#pragma unroll
  for (int n = 0; n < 4; ++n)
    __builtin_amdgcn_raw_buffer_store_b128(ob[n], w.ro, w.vo_out + T * w.xstep + n * 4096u, 0, T == 3 ? 0 : AUX_SC1);
}

__global__ __launch_bounds__(256) __attribute__((amdgpu_waves_per_eu(2, 2))) void sliding_kernel(
    const void* __restrict__ q, const void* __restrict__ k, const void* __restrict__ v, void* __restrict__ o) {
  extern __shared__ __attribute__((aligned(16))) char lds[];
  Wave w;
  const u32 lane = threadIdx.x & 63;
  const u32 r = __builtin_amdgcn_readfirstlane(threadIdx.x >> 6);
  const u32 b = (blockIdx.x & 7) * (NBLK / 8) + (blockIdx.x >> 3);
  const bool first = (b & 7) == 0;
  const bool down = (b & 1) != 0;
  const u32 span = b * 65536u;
  const u32 wbase = span + r * 256u;
  w.lbase = (u32)(size_t)(LDS_AS char*)lds + r * WAVE_LDS;
  w.xstep = down ? 0u - TILE_G : TILE_G;
  w.qbase = down ? wbase + 3 * TILE_G : wbase;
  w.xbase = down ? wbase + 3 * TILE_G : wbase - TILE_G;
  w.x0base = down ? wbase + 3 * TILE_G : (first ? span : wbase - TILE_G);

  const u32 dq = lane >> 4, dc = lane & 15;
#pragma unroll
  for (int m = 0; m < 4; ++m) {
    const u32 ck = (dc ^ (4 * m + dq)) * 16, cv = (dc ^ ((dq << 2) | m)) * 16;
    w.vo_k[m] = dq * 1024 + 3072 * m + ck;
    w.vo_v[m] = dq * 1024 + 3072 * m + cv;
    const u32 rowoff = (!down && m < 2) ? OOB : ((!down && first) ? 0u : dq * 1024 + 4096 * m);
    w.x0_k[m] = rowoff + ck;
    w.x0_v[m] = rowoff + cv;
    if (m < 2) {
      w.x4_k[m] = down ? OOB : w.vo_k[m];
      w.x4_v[m] = down ? OOB : w.vo_v[m];
    }
  }
  asm volatile("" ::"v"(w.vo_k[0]), "v"(w.vo_k[1]), "v"(w.vo_k[2]), "v"(w.vo_k[3]), "v"(w.x0_k[0]), "v"(w.x0_k[1]),
               "v"(w.x0_k[2]), "v"(w.x0_k[3]));
  __builtin_amdgcn_sched_barrier(0);

  w.rq = u32x4{(u32)(size_t)q, (u32)((size_t)q >> 32) & 0xffffu, TENSOR_BYTES, 0x00020000u};
  w.rk = u32x4{(u32)(size_t)k, (u32)((size_t)k >> 32) & 0xffffu, TENSOR_BYTES, 0x00020000u};
  w.rv = u32x4{(u32)(size_t)v, (u32)((size_t)v >> 32) & 0xffffu, TENSOR_BYTES, 0x00020000u};
  w.ro = __builtin_amdgcn_make_buffer_rsrc(o, 0, TENSOR_BYTES, 0x00020000);

  asm volatile("s_nop 4" ::"s"(w.rq), "s"(w.rk), "s"(w.rv) : "memory");
  issue_group<0>(w);
  issue_group<1>(w);
  issue_group<2>(w);

  const u32 i = lane & 15, g = lane >> 4;
  const u32 tq = i >> 2, tp = i & 3;
#pragma unroll
  for (int x = 0; x < 4; ++x) {
    w.a_row[x] = w.lbase + i * 256 + (((g ^ i) * 16) ^ (64 * x));
    w.a_tr[x] = w.lbase + OFF_V + (4 * g + tq) * 256 + (((x ^ tq) << 6) | ((tp ^ g) << 4));
  }
#pragma unroll
  for (int x = 0; x < 2; ++x) {
    w.a_ow[x] = w.lbase + OFF_O + i * 256 + ((((x ^ (i >> 2)) & 1) << 6) | ((g ^ (i & 3)) << 4));
    w.a_or[x] = w.lbase + OFF_O + dq * 256 + (((dc ^ dq) * 16) ^ (64 * x));
  }
  w.vo_out = w.qbase + dq * 1024 + dc * 16;
  const float NEG_INF = -__builtin_inff();
#pragma unroll
  for (int e = 0; e < 4; ++e) {
    const int kb = 4 * g + e;
    const bool in_cur = kb <= (int)i && kb + 7 >= (int)i;
    const bool in_prev = kb >= (int)i + 9;
    w.bias_new[e] = (down ? in_prev : in_cur) ? 0.f : NEG_INF;
    w.bias_old[e] = (down ? in_cur : in_prev) ? 0.f : NEG_INF;
    w.bias_old0[e] = (down ? in_cur : in_prev) ? ((!down && first) ? MASK_LOG2E : 0.f) : NEG_INF;
  }
  asm volatile("" ::"v"(w.a_row[0]), "v"(w.a_row[1]), "v"(w.a_row[2]), "v"(w.a_row[3]), "v"(w.a_tr[0]),
               "v"(w.a_tr[1]), "v"(w.a_tr[2]), "v"(w.a_tr[3]), "v"(w.a_ow[0]), "v"(w.a_ow[1]), "v"(w.a_or[0]),
               "v"(w.a_or[1]), "v"(w.vo_out));
  asm volatile("" ::"v"(w.bias_new[0]), "v"(w.bias_new[1]), "v"(w.bias_new[2]), "v"(w.bias_new[3]),
               "v"(w.bias_old[0]), "v"(w.bias_old[1]), "v"(w.bias_old[2]), "v"(w.bias_old[3]), "v"(w.bias_old0[0]),
               "v"(w.bias_old0[1]), "v"(w.bias_old0[2]), "v"(w.bias_old0[3]));
  __builtin_amdgcn_sched_barrier(0);

  tile<0>(w);
  tile<1>(w);
  tile<2>(w);
  tile<3>(w);
}

extern "C" void kernel_launch(void* const* d_in, const int* in_sizes, int n_in, void* d_out, int out_size, void* d_ws,
                              size_t ws_size, hipStream_t stream) {
  (void)hipFuncSetAttribute((const void*)sliding_kernel, hipFuncAttributeMaxDynamicSharedMemorySize, 4 * WAVE_LDS);
  sliding_kernel<<<NBLK, 256, 4 * WAVE_LDS, stream>>>(d_in[0], d_in[1], d_in[2], d_out);
}
```
